```python
import torch

def _softmax(x, axis=-1):
    return torch.softmax(x, dim=axis)
def _gelu(x):
    return torch.nn.functional.gelu(x, approximate="tanh")
def _silu(x):
    return torch.nn.functional.silu(x)
def _sigmoid(x):
    return torch.sigmoid(x)
def _layernorm(x, axis=-1, eps=1e-5):
    m = x.mean(dim=axis, keepdim=True)
    v = x.var(dim=axis, keepdim=True, unbiased=False)
    return (x - m) / torch.sqrt(v + eps)
def _scatter_add(tgt, idx, src, axis=0):
    return tgt.clone().index_add_(axis, idx, src)
def _segment_sum(src, seg, n):
    out = torch.zeros((n,) + src.shape[1:], dtype=src.dtype)
    return out.index_add_(0, seg, src)
def _pad(x, pw):
    flat = tuple(v for pair in reversed(pw) for v in pair)
    return torch.nn.functional.pad(x, flat)

kernel_name = 'standalone__mha_softmax__sampling'

def setup_inputs():

    _w = (lambda a: (lambda t: t.to(torch.bfloat16) if t.is_floating_point() else t)(torch.as_tensor(a)))
    B, H, Sq, Sk, D = 8, 32, 1, 8192, 128
    return {
        "q": _w((lambda *s: torch.randn(*s, dtype=torch.bfloat16) * 0.8)(B, H, Sq, D)),
        "k": _w((lambda *s: torch.randn(*s, dtype=torch.bfloat16) * 0.8)(B, H, Sk, D)),
        "v": _w((lambda *s: torch.randn(*s, dtype=torch.bfloat16) * 0.05)(B, H, Sk, D)),
    }

def reference(q, k, v):
    scale = q.shape[-1] ** -0.5
    scores = torch.einsum("bhqd,bhkd->bhqk", q, k) * scale
    p = _softmax(scores, axis=-1)
    return torch.einsum("bhqk,bhkd->bhqd", p, v)
```

```cpp
#include <hip/hip_runtime.h>
#include <hip/hip_bf16.h>
#include <stdint.h>

typedef unsigned u32x4 __attribute__((ext_vector_type(4)));
typedef float f32x4 __attribute__((ext_vector_type(4)));
typedef __bf16 bf16x2 __attribute__((ext_vector_type(2)));

constexpr int NB = 8, NH = 32, SK = 8192, HD = 128;
constexpr int NHEADS = NB * NH;
constexpr int HEAD_BYTES = SK * HD * 2;
#define NW 16
#define NL 8
#define AUX 2
constexpr int TILE_BYTES = NL * 1024;
constexpr int NT = HEAD_BYTES / TILE_BYTES / NW;

__device__ __forceinline__ float dot2(unsigned a, unsigned b, float acc) {
    return __builtin_amdgcn_fdot2_f32_bf16(__builtin_bit_cast(bf16x2, a), __builtin_bit_cast(bf16x2, b), acc, false);
}
template <int CTRL> __device__ __forceinline__ float dpp(float x) {
    return __builtin_bit_cast(float, __builtin_amdgcn_mov_dpp(__builtin_bit_cast(int, x), CTRL, 0xf, 0xf, true));
}
constexpr int XOR1 = 0xB1, XOR2 = 0x4E, XOR7 = 0x141, XOR8 = 0x128, ROR4 = 0x124, ROR2 = 0x122, ROR1 = 0x121, BCAST = 0x150;

__device__ __forceinline__ float xrow_max(float x) {
    auto s = __builtin_amdgcn_permlane16_swap(__float_as_uint(x), __float_as_uint(x), false, false);
    x = fmaxf(__uint_as_float(s[0]), __uint_as_float(s[1]));
    auto t = __builtin_amdgcn_permlane32_swap(__float_as_uint(x), __float_as_uint(x), false, false);
    return fmaxf(__uint_as_float(t[0]), __uint_as_float(t[1]));
}
__device__ __forceinline__ float xrow_sum(float x) {
    auto s = __builtin_amdgcn_permlane16_swap(__float_as_uint(x), __float_as_uint(x), false, false);
    x = __uint_as_float(s[0]) + __uint_as_float(s[1]);
    auto t = __builtin_amdgcn_permlane32_swap(__float_as_uint(x), __float_as_uint(x), false, false);
    return __uint_as_float(t[0]) + __uint_as_float(t[1]);
}

template <int I> struct PV {
    static __device__ __forceinline__ void run(float (&acc)[8], const u32x4& vv, float p) {
        constexpr int SRC = I * (16 / NL);
        const float pb = dpp<BCAST + SRC>(p);
#pragma unroll
        for (int k = 0; k < 4; ++k) {
            const float lo = __builtin_bit_cast(float, vv[k] << 16);
            const float hi = __builtin_bit_cast(float, vv[k] & 0xffff0000u);
            acc[2 * k] = fmaf(pb, lo, acc[2 * k]);
            acc[2 * k + 1] = fmaf(pb, hi, acc[2 * k + 1]);
        }
    }
};

template <int I, int N> struct PVLoop {
    template <typename R>
    static __device__ __forceinline__ void run(float (&acc)[8], u32x4 (&vt)[NL], float p, R rsrc, int voff, int soff) {
        PV<I>::run(acc, vt[I], p);
        asm volatile("" : "+v"(acc[0]), "+v"(acc[1]), "+v"(acc[2]), "+v"(acc[3]), "+v"(acc[4]), "+v"(acc[5]), "+v"(acc[6]), "+v"(acc[7]) : : "memory");
        vt[I] = __builtin_amdgcn_raw_buffer_load_b128(rsrc, voff + (I & 3) * 1024, soff + (I >> 2) * 4096, AUX);
        __builtin_amdgcn_sched_barrier(0);
        PVLoop<I + 1, N>::run(acc, vt, p, rsrc, voff, soff);
    }
};
template <int N> struct PVLoop<N, N> {
    template <typename R>
    static __device__ __forceinline__ void run(float (&)[8], u32x4 (&)[NL], float, R, int, int) {}
};

__global__ void __launch_bounds__(NW * 64) attn_decode(const __hip_bfloat16* __restrict__ q, const __hip_bfloat16* __restrict__ k,
                                                       const __hip_bfloat16* __restrict__ v, __hip_bfloat16* __restrict__ out) {
    __shared__ __attribute__((aligned(16))) float lds[NW * 132];
    const int head = blockIdx.x;
    const int tid = threadIdx.x, lane = tid & 63, wave = __builtin_amdgcn_readfirstlane(tid >> 6);
    const int c = lane & 15;
    constexpr float C2 = 0.088388347648318440f * 1.4426950408889634f;

    const auto rk = __builtin_amdgcn_make_buffer_rsrc((void*)((const char*)k + (size_t)head * HEAD_BYTES), 0, HEAD_BYTES, 0x00020000);
    const auto rv = __builtin_amdgcn_make_buffer_rsrc((void*)((const char*)v + (size_t)head * HEAD_BYTES), 0, HEAD_BYTES, 0x00020000);
    const u32x4 qv = *reinterpret_cast<const u32x4*>(q + head * HD + 8 * c);
    const int voff = lane * 16;

    u32x4 kt[NL], vt[NL];
    int soff = wave * TILE_BYTES;
#pragma unroll
    for (int i = 0; i < NL; ++i) kt[i] = __builtin_amdgcn_raw_buffer_load_b128(rk, voff + (i & 3) * 1024, soff + (i >> 2) * 4096, AUX);
#pragma unroll
    for (int i = 0; i < NL; ++i) vt[i] = __builtin_amdgcn_raw_buffer_load_b128(rv, voff + (i & 3) * 1024, soff + (i >> 2) * 4096, AUX);
    __builtin_amdgcn_sched_barrier(0);

    float m = -3.0e38f, l = 0.f;
    float acc[8];
#pragma unroll
    for (int i = 0; i < 8; ++i) acc[i] = 0.f;

    for (int j = 0; j < NT; ++j) {
        soff += NW * TILE_BYTES;
        float x[NL];
#pragma unroll
        for (int i = 0; i < NL; ++i) {
            float d = dot2(kt[i][0], qv[0], 0.f);
            d = dot2(kt[i][1], qv[1], d);
            d = dot2(kt[i][2], qv[2], d);
            d = dot2(kt[i][3], qv[3], d);
            x[i] = d;
            kt[i] = __builtin_amdgcn_raw_buffer_load_b128(rk, voff + (i & 3) * 1024, soff + (i >> 2) * 4096, AUX);
            __builtin_amdgcn_sched_barrier(0);
        }
#define TR_STEP(HALF, CTRL, BIT)                                                        \
        if constexpr ((HALF) >= 1) {                                                    \
            _Pragma("unroll") for (int h = 0; h < (HALF); ++h) {                        \
                const float lo_ = x[h], hi_ = x[h + (HALF)];                            \
                const float t1_ = lo_ + dpp<CTRL>(lo_), t2_ = hi_ + dpp<CTRL>(hi_);     \
                x[h] = (c & (BIT)) ? t2_ : t1_; }                                       \
        } else { x[0] = x[0] + dpp<CTRL>(x[0]); }
        TR_STEP(NL / 2, XOR8, 8) TR_STEP(NL / 4, XOR7, 4) TR_STEP(NL / 8, XOR2, 2) TR_STEP(NL / 16, XOR1, 1)
#undef TR_STEP
        const float s = x[0] * C2;
        float pm = fmaxf(s, dpp<XOR8>(s));
        pm = fmaxf(pm, dpp<ROR4>(pm));
        if constexpr (NL >= 8) pm = fmaxf(pm, dpp<ROR2>(pm));
        if constexpr (NL >= 16) pm = fmaxf(pm, dpp<ROR1>(pm));
        const float mn = fmaxf(m, pm);
        const float alpha = __builtin_amdgcn_exp2f(m - mn);
        const float p = __builtin_amdgcn_exp2f(s - mn);
        m = mn;
        l = fmaf(l, alpha, p);
#pragma unroll
        for (int i = 0; i < 8; ++i) acc[i] *= alpha;
        PVLoop<0, NL>::run(acc, vt, p, rv, voff, soff);
    }

    const float mw = xrow_max(m);
    const float f = __builtin_amdgcn_exp2f(m - mw);
    l *= f;
    l += dpp<XOR8>(l); l += dpp<ROR4>(l);
    if constexpr (NL >= 8) l += dpp<ROR2>(l);
    if constexpr (NL >= 16) l += dpp<ROR1>(l);
    l = xrow_sum(l);
#pragma unroll
    for (int i = 0; i < 8; ++i) acc[i] = xrow_sum(acc[i] * f);
    float* rec = lds + wave * 132;
    if (lane < 16) {
        *reinterpret_cast<f32x4*>(rec + 4 + 8 * c) = f32x4{acc[0], acc[1], acc[2], acc[3]};
        *reinterpret_cast<f32x4*>(rec + 4 + 8 * c + 4) = f32x4{acc[4], acc[5], acc[6], acc[7]};
        if (lane == 0) { rec[0] = mw; rec[1] = l; }
    }
    __syncthreads();
    if (wave == 0) {
        float M = lds[0];
#pragma unroll
        for (int w = 1; w < NW; ++w) M = fmaxf(M, lds[w * 132]);
        float L = 0.f, o0 = 0.f, o1 = 0.f;
#pragma unroll
        for (int w = 0; w < NW; ++w) {
            const float wt = __builtin_amdgcn_exp2f(lds[w * 132] - M);
            L = fmaf(wt, lds[w * 132 + 1], L);
            const float2 a = *reinterpret_cast<const float2*>(lds + w * 132 + 4 + 2 * lane);
            o0 = fmaf(wt, a.x, o0); o1 = fmaf(wt, a.y, o1);
        }
        const float inv = 1.f / L;
        __hip_bfloat162 r;
        r.x = __float2bfloat16(o0 * inv); r.y = __float2bfloat16(o1 * inv);
        *reinterpret_cast<__hip_bfloat162*>(out + head * HD + 2 * lane) = r;
    }
}

extern "C" void kernel_launch(void* const* d_in, const int* in_sizes, int n_in, void* d_out, int out_size, void* d_ws, size_t ws_size,
                              hipStream_t stream) {
    const __hip_bfloat16* q = (const __hip_bfloat16*)d_in[0];
    const __hip_bfloat16* k = (const __hip_bfloat16*)d_in[1];
    const __hip_bfloat16* v = (const __hip_bfloat16*)d_in[2];
    attn_decode<<<NHEADS, NW * 64, 0, stream>>>(q, k, v, (__hip_bfloat16*)d_out);
}
```

```cpp
#include <hip/hip_runtime.h>
#include <hip/hip_bf16.h>
#include <stdint.h>

typedef unsigned u32x4 __attribute__((ext_vector_type(4)));
typedef float f32x4 __attribute__((ext_vector_type(4)));
typedef float f32x2 __attribute__((ext_vector_type(2)));
typedef __bf16 bf16x2 __attribute__((ext_vector_type(2)));
#define LAS __attribute__((address_space(3)))

constexpr int NB = 8, NH = 32, SK = 8192, HD = 128;
constexpr int NHEADS = NB * NH;
constexpr int HEAD_BYTES = SK * HD * 2;
constexpr int NC = 4;
constexpr double RATE_TBS = 8.00;
constexpr int START = 75, V_GAP = 24;
constexpr int FLY = 40, FLY_WIDE = 48;
constexpr int TOL = 12, TOL_LOW = 30;
constexpr int LOW_CLK = 18;
constexpr int SLACK0 = -36, SLACK_CLIP = 16, SLACK_THR = -20, SLACK_STEP = 4;
constexpr int STALL = 90, STALL_DEBT = 5;
constexpr int MAX_DEBT = 3, LEAD = 3;
constexpr int NSLOT = 7;
constexpr int BL = 16;
constexpr int NBURST = 2 * HEAD_BYTES / (BL * 1024);
constexpr int NPH = NBURST / 2;
constexpr double PERIOD = (double)(BL * 1024) * NHEADS / (RATE_TBS * 1e12) * 1e8;
constexpr unsigned PER256 = (unsigned)(PERIOD * 256 + 0.5);
constexpr unsigned PERI = (unsigned)PERIOD, PERF = (unsigned)((PERIOD - PERI) * 4294967296.0);
constexpr int L_RING = 0, L_SC = NSLOT * BL * 1024, L_MRG = L_SC + SK * 4, L_FLG = L_MRG + NC * 512, L_END = L_FLG + 128 + NBURST * 4 + 16;
constexpr int F_LANDED = 0, F_DONE = 16, F_MXF = 32, F_LF = 48, F_ACCF = 64, F_MX = 80, F_L = 96, F_IDX = 128;

__device__ __forceinline__ float dot2(unsigned a, unsigned b, float acc) {
    return __builtin_amdgcn_fdot2_f32_bf16(__builtin_bit_cast(bf16x2, a), __builtin_bit_cast(bf16x2, b), acc, false);
}
template <int CTRL> __device__ __forceinline__ float dpp(float x) {
    return __builtin_bit_cast(float, __builtin_amdgcn_mov_dpp(__builtin_bit_cast(int, x), CTRL, 0xf, 0xf, true));
}
constexpr int XOR1 = 0xB1, XOR2 = 0x4E, XOR7 = 0x141, XOR8 = 0x128, ROR4 = 0x124, ROR2 = 0x122, ROR1 = 0x121;

__device__ __forceinline__ float swap32_add(float a, float b) {
    auto s = __builtin_amdgcn_permlane32_swap(__float_as_uint(a), __float_as_uint(b), false, false);
    return __uint_as_float(s[0]) + __uint_as_float(s[1]);
}
__device__ __forceinline__ float swap16_add(float a, float b) {
    auto s = __builtin_amdgcn_permlane16_swap(__float_as_uint(a), __float_as_uint(b), false, false);
    return __uint_as_float(s[0]) + __uint_as_float(s[1]);
}
__device__ __forceinline__ float xrow_max(float x) {
    auto s = __builtin_amdgcn_permlane16_swap(__float_as_uint(x), __float_as_uint(x), false, false);
    x = fmaxf(__uint_as_float(s[0]), __uint_as_float(s[1]));
    auto t = __builtin_amdgcn_permlane32_swap(__float_as_uint(x), __float_as_uint(x), false, false);
    return fmaxf(__uint_as_float(t[0]), __uint_as_float(t[1]));
}
template <int N> __device__ __forceinline__ void wait_vm() { asm volatile("s_waitcnt vmcnt(%0)" : : "i"(N) : "memory"); }
__device__ __forceinline__ void flag_put(unsigned addr, unsigned val) { asm volatile("ds_write_b32 %0, %1" : : "v"(addr), "v"(val) : "memory"); }
__device__ __forceinline__ unsigned flag_get(unsigned addr) {
    unsigned r; asm volatile("ds_read_b32 %0, %1\n\ts_waitcnt lgkmcnt(0)" : "=&v"(r) : "v"(addr) : "memory");
    return __builtin_amdgcn_readfirstlane(r);
}
__device__ __forceinline__ unsigned flag_min4(unsigned addr) {
    u32x4 r; asm volatile("ds_read_b128 %0, %1\n\ts_waitcnt lgkmcnt(0)" : "=&v"(r) : "v"(addr) : "memory");
    return __builtin_amdgcn_readfirstlane(min(min(r[0], r[1]), min(r[2], r[3])));
}

__device__ __forceinline__ unsigned blk_off(unsigned j) {
    return ((j & 1) << 11) ^ ((j & 2) << 14) ^ ((j & 4) << 15) ^ ((j & 8) << 17) ^ ((j & 16) << 10) ^ ((j & 32) << 14) ^ ((j & 32) << 9) ^ ((j & 64) << 7);
}
constexpr unsigned long long XM_LO = 0xd08f8212dd57f86bull, XM_HI = 0x66245full;

__global__ void __launch_bounds__(NC * 64 + 64) attn_decode(const __hip_bfloat16* __restrict__ q, const __hip_bfloat16* __restrict__ k,
                                                            const __hip_bfloat16* __restrict__ v, __hip_bfloat16* __restrict__ out) {
    __shared__ __attribute__((aligned(16))) char lds[L_END];
    unsigned long long kp, vp, qp, t64, c64;
    asm volatile("s_load_dwordx2 %0, %5, 0x8\n\ts_load_dwordx2 %1, %5, 0x10\n\ts_load_dwordx2 %2, %5, 0x0\n\ts_memrealtime %3\n\ts_memtime %4"
                 : "=&s"(kp), "=&s"(vp), "=&s"(qp), "=&s"(t64), "=&s"(c64) : "s"(__builtin_amdgcn_kernarg_segment_ptr()));
    __builtin_amdgcn_sched_barrier(0);
    const int head = blockIdx.x;
    const int tid = threadIdx.x, lane = tid & 63, wave = __builtin_amdgcn_readfirstlane(tid >> 6);
    const unsigned fl = (unsigned)(size_t)(LAS char*)lds + L_FLG;
    u32x4 qv;
    asm volatile("s_cmp_eq_u32 %[w], 0\n\ts_cbranch_scc1 1f\n\t"
                 "s_getpc_b64 vcc\n\tglobal_load_dword v57, %[o64], vcc\n\t"
                 "s_waitcnt lgkmcnt(0)\n\t"
                 "global_load_dword v57, %[oh], %[kp] nt\n\tglobal_load_dword v57, %[oh], %[vp] nt\n\tglobal_load_dwordx4 %[q], %[oq], %[qp]\n\t"
                 "s_barrier\n\ts_waitcnt vmcnt(0)\n"
                 "1:"
                 : [q] "=&v"(qv)
                 : [kp] "s"(kp), [vp] "s"(vp), [qp] "s"(qp), [t] "s"(t64), [c] "s"(c64), [w] "s"(wave), [o64] "v"(lane * 64),
                   [oh] "v"((unsigned)head * HEAD_BYTES + ((head * 37) & 255) * 128), [oq] "v"(head * (HD * 2) + (lane & 15) * 16)
                 : "memory", "scc", "vcc", "v57");

    if (wave == 0) {
        const unsigned hm = (unsigned)((lane & 8 ? XM_HI : XM_LO) >> ((lane & 7) * 8)) & head;
        const unsigned xm = ((unsigned)__ballot(__builtin_popcount(hm & 255u) & 1) & 0x7ffu) << 10;
        constexpr unsigned VM = (1u << 12) | (1u << 18);
        const unsigned xr = xm & ~VM;
        int voff[4];
#pragma unroll
        for (int i = 0; i < 4; ++i) voff[i] = lane * 16 + ((((i & 1) << 18) | ((i >> 1) << 12)) ^ (xm & VM)) + 3072 - i * 1024;
        flag_put(fl + (lane & 31) * 4, (lane & 28) == F_DONE / 4 ? NSLOT : 0u);
        asm volatile("ds_write_b128 %0, %1" : : "v"(fl + F_IDX + lane * 16), "v"(u32x4{0u, 0u, 0u, 0u}) : "memory");
        const unsigned tab = xr ^ blk_off(lane);
        const unsigned phase = PER256 - (unsigned)(head ^ ((head & 1) << 4)) * PER256 / NHEADS;
        const unsigned long long hoff = (unsigned long long)head * HEAD_BYTES - 3072;
        unsigned dsto = (unsigned)(size_t)(LAS char*)lds + L_RING;
        const unsigned dend = dsto + NSLOT * BL * 1024;
        __builtin_amdgcn_s_setprio(3);
        __builtin_amdgcn_sched_barrier(0);
        asm volatile("s_waitcnt lgkmcnt(0)" : "+s"(kp), "+s"(vp), "+s"(qp), "+s"(t64), "+s"(c64) : : "memory");
        const unsigned long long ka = kp + hoff, va = vp + hoff;
        const unsigned t0 = (unsigned)t64;
        unsigned cyc = (unsigned)c64 - t0 * LOW_CLK;
        const unsigned a0 = (((t0 + START) & 0x7fffffu) << 8) + phase, q0 = a0 / PER256;
        const unsigned back = __builtin_amdgcn_readfirstlane(a0 - q0 * PER256);
        unsigned rnd = __builtin_amdgcn_readfirstlane(q0 + 0x3fffffffu);
        unsigned tint = t0 + START - (back >> 8) - 1, tfrac = (255u - (back & 255u)) << 24;
        unsigned j, pend, cur, base, late, maxd, sm, thr, cnt, debt, spec, fin, win, t1, t2, t3;
        asm volatile(
            "s_mov_b32 s90, %[nrec]\n\t"
            "s_mov_b32 s91, 0x20000\n\t"
            "s_mov_b32 s88, %[klo]\n\t"
            "s_mov_b32 s89, %[khi]\n\t"
            "s_mov_b32 %[cur], %[rnd]\n\t"
            "s_mov_b32 %[j], 0\n\t"
            "s_mov_b32 %[late], 0\n\t"
            "s_add_u32 %[pend], %[rnd], 128\n\t"
            "s_mov_b32 %[cnt], 0\n\t"
            "s_mov_b32 %[maxd], %[maxd2]\n\t"
            "s_mov_b32 %[sm], %[sm0]\n\t"
            "s_mov_b32 %[thr], %[thr0]\n\t"
            "s_mov_b32 %[debt], 0\n\t"
            "s_mov_b32 %[spec], 1\n\t"
            "s_mov_b32 %[fin], %[nb]\n\t"
            "s_mov_b32 %[win], 0\n\t"
            "v_mov_b32 v62, %[aidx]\n\t"
            "v_mov_b32 v63, %[land0]\n"
            "4:\n\t"
            "v_readlane_b32 %[base], %[tab], %[cur]\n\t"
            "s_lshl_b32 %[t1], %[cur], 7\n\t"
            "s_and_b32 %[t1], %[t1], 0x2000\n\t"
            "s_xor_b32 %[base], %[base], %[t1]\n"
            "2:\n\t"
            "s_cmp_ge_i32 %[late], 0\n\t"
            "s_cbranch_scc1 0f\n\t"
            "s_memrealtime vcc\n\t"
            "s_waitcnt lgkmcnt(0)\n\t"
            "s_sub_i32 %[late], vcc_lo, %[tint]\n\t"
            "s_branch 2b\n"
            "0:\n\t"
            "s_mov_b32 m0, %[dsto]\n\t"
            "s_xor_b32 %[t1], %[base], 0x400\n\t"
            "buffer_load_dwordx4 %[v0], s[88:91], %[base] offen nt lds\n\t"
            "buffer_load_dwordx4 %[v1], s[88:91], %[base] offen offset:1024 nt lds\n\t"
            "buffer_load_dwordx4 %[v2], s[88:91], %[base] offen offset:2048 nt lds\n\t"
            "buffer_load_dwordx4 %[v3], s[88:91], %[base] offen offset:3072 nt lds\n\t"
            "s_add_u32 m0, %[dsto], 0x1000\n\t"
            "s_xor_b32 %[t2], %[base], 0x10000\n\t"
            "buffer_load_dwordx4 %[v0], s[88:91], %[t1] offen nt lds\n\t"
            "buffer_load_dwordx4 %[v1], s[88:91], %[t1] offen offset:1024 nt lds\n\t"
            "buffer_load_dwordx4 %[v2], s[88:91], %[t1] offen offset:2048 nt lds\n\t"
            "buffer_load_dwordx4 %[v3], s[88:91], %[t1] offen offset:3072 nt lds\n\t"
            "s_add_u32 m0, %[dsto], 0x2000\n\t"
            "s_xor_b32 %[t3], %[base], 0x10400\n\t"
            "buffer_load_dwordx4 %[v0], s[88:91], %[t2] offen nt lds\n\t"
            "buffer_load_dwordx4 %[v1], s[88:91], %[t2] offen offset:1024 nt lds\n\t"
            "buffer_load_dwordx4 %[v2], s[88:91], %[t2] offen offset:2048 nt lds\n\t"
            "buffer_load_dwordx4 %[v3], s[88:91], %[t2] offen offset:3072 nt lds\n\t"
            "s_add_u32 m0, %[dsto], 0x3000\n\t"
            "s_add_u32 %[rnd], %[rnd], 1\n\t"
            "buffer_load_dwordx4 %[v0], s[88:91], %[t3] offen nt lds\n\t"
            "buffer_load_dwordx4 %[v1], s[88:91], %[t3] offen offset:1024 nt lds\n\t"
            "buffer_load_dwordx4 %[v2], s[88:91], %[t3] offen offset:2048 nt lds\n\t"
            "buffer_load_dwordx4 %[v3], s[88:91], %[t3] offen offset:3072 nt lds\n\t"
            "v_mov_b32 v61, %[cur]\n\t"
            "v_add_u32 v63, 16, v63\n\t"
            "s_cmp_lg_u32 %[win], 0\n\t"
            "ds_write_b32 v62, v61\n\t"
            "s_cbranch_scc1 1f\n\t"
            "s_waitcnt vmcnt(%[w0])\n\t"
            "ds_write_b32 %[vla], v63\n"
            "12:\n\t"
            "ds_read_b32 v60, %[vda]\n\t"
            "s_memrealtime vcc\n\t"
            "s_memtime s[92:93]\n\t"
            "v_readlane_b32 %[base], %[tab], %[rnd]\n\t"
            "v_add_u32 v62, 4, v62\n\t"
            "s_add_u32 %[tfrac], %[tfrac], %[pf]\n\t"
            "s_addc_u32 %[tint], %[tint], %[pi]\n\t"
            "s_add_u32 %[dsto], %[dsto], 0x4000\n\t"
            "s_lshl_b32 %[t1], %[rnd], 7\n\t"
            "s_cmp_eq_u32 %[dsto], %[dend]\n\t"
            "s_cselect_b32 %[dsto], %[ring0], %[dsto]\n\t"
            "s_and_b32 %[t1], %[t1], 0x2000\n\t"
            "s_add_u32 %[j], %[j], 1\n\t"
            "s_xor_b32 %[base], %[base], %[t1]\n\t"
            "s_mov_b32 %[cur], %[rnd]\n"
            "5:\n\t"
            "s_waitcnt lgkmcnt(0)\n\t"
            "s_sub_i32 %[late], vcc_lo, %[tint]\n\t"
            "s_mul_i32 %[t2], vcc_lo, %[cpt]\n\t"
            "v_cmp_ge_i32 vcc, %[j], v60\n\t"
            "s_sub_u32 %[t2], s92, %[t2]\n\t"
            "s_sub_i32 %[t1], %[late], %[sm]\n\t"
            "s_cmp_lg_u32 vcc_lo, 0\n\t"
            "s_cbranch_scc1 51f\n\t"
            "s_min_i32 %[t1], %[t1], %[clip]\n\t"
            "s_ashr_i32 %[t1], %[t1], 2\n\t"
            "s_add_i32 %[sm], %[sm], %[t1]\n\t"
            "s_sub_i32 %[t1], %[t2], %[cyc]\n\t"
            "s_mov_b32 %[cyc], %[t2]\n\t"
            "s_lshr_b32 %[win], %[t1], 31\n\t"
            "s_cselect_b32 %[sm], %[sm0], %[sm]\n\t"
            "s_cselect_b32 %[t1], %[tollow], %[tol0]\n\t"
            "s_cmp_gt_i32 %[late], %[t1]\n\t"
            "s_cbranch_scc1 6f\n\t"
            "s_cmp_gt_i32 %[sm], %[thr]\n\t"
            "s_cbranch_scc1 6f\n\t"
            "s_cmp_lt_u32 %[j], %[spec]\n\t"
            "s_cbranch_scc1 2b\n"
            "3:\n\t"
            "s_cmp_eq_u32 %[spec], 1\n\t"
            "s_cbranch_scc1 8f\n\t"
            "s_cmp_ge_u32 %[j], %[fin]\n\t"
            "s_cselect_b32 %[late], 0, %[late]\n\t"
            "s_cselect_b32 %[win], 1, %[win]\n\t"
            "s_cmp_lt_u32 %[j], %[nb3]\n\t"
            "s_cbranch_scc1 31f\n\t"
            "v_add_u32 v61, %[w0] - 16, v63\n\t"
            "s_mov_b32 %[win], 0\n\t"
            "s_waitcnt vmcnt(16)\n\t"
            "ds_write_b32 %[vla], v61\n"
            "31:\n\t"
            "s_cmp_lt_u32 %[rnd], %[pend]\n\t"
            "s_cbranch_scc1 32f\n\t"
            "s_cmp_eq_u32 %[cnt], 0\n\t"
            "s_cbranch_scc1 7f\n\t"
            "s_sub_u32 %[cnt], %[cnt], 1\n\t"
            "s_mov_b32 %[spec], 0\n\t"
            "v_readlane_b32 %[cur], v58, %[cnt]\n\t"
            "s_nop 3\n\t"
            "s_branch 4b\n"
            "7:\n\t"
            "s_cmp_eq_u32 %[j], %[nb]\n\t"
            "s_cbranch_scc1 99f\n\t"
            "s_mov_b32 s88, %[vlo]\n\t"
            "s_mov_b32 s89, %[vhi]\n\t"
            "s_add_u32 %[pend], %[rnd], 128\n\t"
            "s_add_u32 %[tint], %[tint], %[vgap]\n\t"
            "s_mov_b32 %[late], -1\n"
            "32:\n\t"
            "s_sub_u32 %[spec], %[pend], %[rnd]\n\t"
            "s_add_u32 %[spec], %[spec], %[j]\n\t"
            "s_min_u32 %[spec], %[spec], %[fin]\n\t"
            "s_min_u32 %[spec], %[spec], %[nb3]\n\t"
            "s_branch 4b\n"
            "1:\n\t"
            "v_add_u32 v61, %[w0] - %[w1], v63\n\t"
            "s_waitcnt vmcnt(%[w1])\n\t"
            "ds_write_b32 %[vla], v61\n\t"
            "s_branch 12b\n"
            "8:\n\t"
            "s_mov_b32 %[spec], 0\n\t"
            "s_barrier\n"
            "51:\n\t"
            "ds_read_b32 v60, %[vda]\n\t"
            "s_memrealtime vcc\n\t"
            "s_memtime s[92:93]\n\t"
            "s_branch 5b\n"
            "6:\n\t"
            "s_mov_b32 %[sm], %[sm0]\n\t"
            "s_add_u32 %[t1], %[j], %[debt]\n\t"
            "s_cmp_ge_u32 %[t1], %[ntail]\n\t"
            "s_cbranch_scc1 3b\n\t"
            "s_cmp_ge_u32 %[rnd], %[pend]\n\t"
            "s_cbranch_scc1 61f\n\t"
            "s_cmp_eq_u32 %[cnt], 64\n\t"
            "s_cbranch_scc1 3b\n\t"
            "s_mov_b32 m0, %[cnt]\n\t"
            "s_add_u32 %[cnt], %[cnt], 1\n\t"
            "v_writelane_b32 v58, %[rnd], m0\n"
            "61:\n\t"
            "s_add_u32 %[rnd], %[rnd], 1\n\t"
            "s_add_u32 %[tfrac], %[tfrac], %[pf]\n\t"
            "s_addc_u32 %[tint], %[tint], %[pi]\n\t"
            "s_add_u32 %[debt], %[debt], 1\n\t"
            "s_add_i32 %[thr], %[thr], %[thk]\n\t"
            "s_add_u32 %[t1], %[j], 2\n\t"
            "s_and_b32 %[t1], %[t1], 127\n\t"
            "s_cmp_lt_u32 %[t1], 12\n\t"
            "s_cselect_b32 %[t1], 0, %[late]\n\t"
            "s_cmp_gt_i32 %[t1], %[bigt]\n\t"
            "s_cselect_b32 %[maxd], 0, %[maxd]\n\t"
            "s_lshl_b32 %[t1], %[maxd], 8\n\t"
            "s_add_u32 %[t1], %[t1], %[debt]\n\t"
            "s_cmp_le_u32 %[t1], %[phi]\n\t"
            "s_cselect_b32 %[t1], 0, %[debt]\n\t"
            "s_min_u32 %[t2], %[t1], %[maxd]\n\t"
            "s_mul_i32 %[t2], %[t2], %[um]\n\t"
            "s_add_u32 %[t1], %[t1], %[t2]\n\t"
            "s_sub_i32 %[fin], %[nb], %[t1]\n\t"
            "s_max_i32 %[fin], %[fin], 2\n\t"
            "s_mov_b32 %[cur], %[rnd]\n\t"
            "s_sub_i32 %[late], %[late], %[pi]\n\t"
            "s_cmp_gt_i32 %[late], %[tol0]\n\t"
            "s_cbranch_scc1 6b\n\t"
            "s_min_i32 %[late], %[late], -1\n\t"
            "s_branch 3b\n"
            "99:"
            : [rnd] "+&s"(rnd), [tfrac] "+&s"(tfrac), [tint] "+&s"(tint), [dsto] "+&s"(dsto), [cyc] "+&s"(cyc), [win] "=&s"(win), [j] "=&s"(j), [pend] "=&s"(pend), [cur] "=&s"(cur),
              [base] "=&s"(base), [late] "=&s"(late), [cnt] "=&s"(cnt), [maxd] "=&s"(maxd), [sm] "=&s"(sm), [thr] "=&s"(thr), [debt] "=&s"(debt), [spec] "=&s"(spec), [fin] "=&s"(fin),
              [t1] "=&s"(t1), [t2] "=&s"(t2), [t3] "=&s"(t3)
            : [v0] "v"(voff[0]), [v1] "v"(voff[1]), [v2] "v"(voff[2]), [v3] "v"(voff[3]), [tab] "v"(tab),
              [vla] "v"(fl + F_LANDED), [vda] "v"(fl + F_DONE + (lane & 3) * 4), [dend] "s"(dend), [ring0] "s"(dend - NSLOT * BL * 1024), [aidx] "s"(fl + F_IDX),
              [klo] "s"((unsigned)ka), [khi] "s"((unsigned)(ka >> 32) & 0xffffu), [vlo] "s"((unsigned)va), [vhi] "s"((unsigned)(va >> 32) & 0xffffu),
              [pf] "i"(PERF), [pi] "i"(PERI),  [w0] "i"(FLY - BL), [w1] "i"(FLY_WIDE - BL), [land0] "i"(BL - FLY),
              [nb] "i"(NBURST), [nb3] "i"(NBURST - 3), [ntail] "i"(NBURST - 1), [nrec] "i"(HEAD_BYTES + 3072),
              [tol0] "i"(TOL),
              [sm0] "i"(SLACK0), [clip] "i"(SLACK_CLIP), [thr0] "i"(SLACK_THR), [thk] "i"(SLACK_STEP),
              [bigt] "i"(STALL), [maxd2] "i"(MAX_DEBT), [phi] "i"(STALL_DEBT), [um] "i"(LEAD), [cpt] "i"(LOW_CLK), [tollow] "i"(TOL_LOW), [vgap] "i"(V_GAP)
            : "memory", "scc", "vcc", "s88", "s89", "s90", "s91", "s92", "s93", "v58", "v60", "v61", "v62", "v63");
        wait_vm<12>(); flag_put(fl + F_LANDED, NBURST * BL - 12);
        wait_vm<8>();  flag_put(fl + F_LANDED, NBURST * BL - 8);
        wait_vm<4>();  flag_put(fl + F_LANDED, NBURST * BL - 4);
        wait_vm<0>();  flag_put(fl + F_LANDED, NBURST * BL);
        asm volatile("s_endpgm");
    }

    const int cw = wave - 1;
    const int c = lane & 15, r = lane >> 4;
    constexpr float C2 = 0.088388347648318440f * 1.4426950408889634f;
    LAS float* sc = (LAS float*)(lds + L_SC) + cw * (SK / NC);
    int landed = 0;
    unsigned ra = fl - L_FLG + L_RING + cw * 4096 + lane * 16;
    const unsigned rend = ra + NSLOT * BL * 1024;
    unsigned idx, wnext = 0;
    u32x4 t[4];
#define GETIDX(j) do { while (wnext == 0u) { wnext = flag_get(fl + F_IDX + (j) * 4); if (wnext == 0u) __builtin_amdgcn_s_sleep(1); } idx = wnext & 127; } while (0)
#define FETCH(j) do {                                                                                                   \
        const int need_ = (j) * BL + cw * 4 + 4;                                                                        \
        while (landed < min(need_, NBURST * BL - 64)) { landed = (int)flag_get(fl + F_LANDED); if (landed < need_) __builtin_amdgcn_s_sleep(1); } \
        do {                                                                                                            \
            unsigned l_, w_;                                                                                            \
            asm volatile("ds_read_b32 %0, %6\n\tds_read_b128 %2, %7\n\tds_read_b128 %3, %7 offset:1024\n\tds_read_b128 %4, %7 offset:2048\n\t"       \
                         "ds_read_b128 %5, %7 offset:3072\n\tds_read_b32 %1, %8\n\ts_waitcnt lgkmcnt(0)"               \
                         : "=&v"(l_), "=&v"(w_), "=&v"(t[0]), "=&v"(t[1]), "=&v"(t[2]), "=&v"(t[3])                     \
                         : "v"(fl + F_LANDED), "v"(ra), "v"(fl + F_IDX + (j) * 4 + 4) : "memory");                      \
            landed = (int)__builtin_amdgcn_readfirstlane(l_); wnext = __builtin_amdgcn_readfirstlane(w_);               \
        } while (landed < need_);                                                                                       \
        flag_put(fl + F_DONE + cw * 4, (unsigned)(j) + 1 + NSLOT);                                                      \
        ra += BL * 1024; ra = (ra == rend) ? ra - NSLOT * BL * 1024 : ra;                                               \
    } while (0)

    float mx = -3.0e38f;
#pragma nounroll
    for (int j = 0; j < NPH; ++j) {
        GETIDX(j);
        FETCH(j);
        float x[4];
#pragma unroll
        for (int i = 0; i < 4; ++i) {
            float d = dot2(t[i][0], qv[0], 0.f);
            d = dot2(t[i][1], qv[1], d);
            d = dot2(t[i][2], qv[2], d);
            d = dot2(t[i][3], qv[3], d);
            x[i] = d;
        }
        const float a0 = x[0] + dpp<XOR8>(x[0]), a1 = x[1] + dpp<XOR8>(x[1]), a2 = x[2] + dpp<XOR8>(x[2]), a3 = x[3] + dpp<XOR8>(x[3]);
        const float b0 = (c & 8) ? a2 : a0, b1 = (c & 8) ? a3 : a1;
        const float c0 = b0 + dpp<XOR7>(b0), c1 = b1 + dpp<XOR7>(b1);
        float s = (c & 4) ? c1 : c0;
        s += dpp<XOR2>(s);
        s = (s + dpp<XOR1>(s)) * C2;
        mx = fmaxf(mx, s);
        sc[idx * 16 + r * 4 + (c >> 2)] = s;
    }
    mx = fmaxf(mx, dpp<XOR8>(mx)); mx = fmaxf(mx, dpp<ROR4>(mx));
    mx = xrow_max(mx);
    flag_put(fl + F_MX + cw * 4, __float_as_uint(mx));
    flag_put(fl + F_MXF + cw * 4, 1u);
    while (flag_min4(fl + F_MXF) == 0u) {}
    {
        const f32x4 m4 = *(const LAS f32x4*)(lds + L_FLG + F_MX);
        mx = fmaxf(fmaxf(m4[0], m4[1]), fmaxf(m4[2], m4[3]));
    }
    float l = 0.f;
    {
        LAS f32x4* ps = (LAS f32x4*)sc + lane;
        f32x4 s4[SK / NC / 256];
#pragma unroll
        for (int i = 0; i < SK / NC / 256; ++i) s4[i] = ps[i * 64];
#pragma unroll
        for (int i = 0; i < SK / NC / 256; ++i) {
#pragma unroll
            for (int e = 0; e < 4; ++e) { s4[i][e] = __builtin_amdgcn_exp2f(s4[i][e] - mx); l += s4[i][e]; }
            ps[i * 64] = s4[i];
        }
    }
    l += dpp<XOR8>(l); l += dpp<ROR4>(l); l += dpp<ROR2>(l); l += dpp<ROR1>(l);
    l = swap16_add(l, l); l = swap32_add(l, l);
    flag_put(fl + F_L + cw * 4, __float_as_uint(l));
    flag_put(fl + F_LF + cw * 4, 1u);
    float inv = 0.f;
    unsigned ooff = (head * HD + 8 * c + 4 * (r >> 1) + 2 * (r & 1)) * 2;
    asm volatile("" : "+v"(ooff));

    double accd[8];
#pragma unroll
    for (int i = 0; i < 8; ++i) accd[i] = 0.0;
    GETIDX(NPH);
    f32x4 pn = *(const LAS f32x4*)(sc + idx * 16 + r * 4);
#pragma nounroll
    for (int j = 0; j < NPH; ++j) {
        if (j == 16 && cw == NC - 1) {
            while (flag_min4(fl + F_LF) == 0u) {}
            const f32x4 l4 = *(const LAS f32x4*)(lds + L_FLG + F_L);
            inv = 1.f / ((l4[0] + l4[1]) + (l4[2] + l4[3]));
        }
        if (wnext == 0u) { GETIDX(NPH + j); pn = *(const LAS f32x4*)(sc + idx * 16 + r * 4); }
        FETCH(NPH + j);
        const f32x4 p4 = pn;
        pn = *(const LAS f32x4*)(sc + (wnext & 127) * 16 + r * 4);
        float part[8];
#pragma unroll
        for (int e = 0; e < 4; ++e) {
            part[2 * e] = p4[0] * __builtin_bit_cast(float, t[0][e] << 16);
            part[2 * e + 1] = p4[0] * __builtin_bit_cast(float, t[0][e] & 0xffff0000u);
        }
#pragma unroll
        for (int i = 1; i < 4; ++i)
#pragma unroll
            for (int e = 0; e < 4; ++e) {
                part[2 * e] = fmaf(p4[i], __builtin_bit_cast(float, t[i][e] << 16), part[2 * e]);
                part[2 * e + 1] = fmaf(p4[i], __builtin_bit_cast(float, t[i][e] & 0xffff0000u), part[2 * e + 1]);
            }
#pragma unroll
        for (int e = 0; e < 8; ++e) accd[e] += (double)part[e];
    }
    float acc[8];
#pragma unroll
    for (int i = 0; i < 8; ++i) acc[i] = (float)accd[i];

    const float h0 = swap32_add(acc[0], acc[4]), h1 = swap32_add(acc[1], acc[5]), h2 = swap32_add(acc[2], acc[6]), h3 = swap32_add(acc[3], acc[7]);
    f32x2 o = {swap16_add(h0, h2), swap16_add(h1, h3)};
    const unsigned ma = fl - (L_FLG - L_MRG) + lane * 8;
    if (cw != NC - 1) {
        asm volatile("ds_write_b64 %0, %1\n\tds_write_b32 %2, %3" : : "v"(ma + cw * 512), "v"(o), "v"(fl + F_ACCF + cw * 4), "v"(1u) : "memory");
        return;
    }
    f32x2 o0, o1, o2;
    for (;;) {
        u32x4 f;
        asm volatile("ds_read_b128 %0, %4\n\tds_read_b64 %1, %5\n\tds_read_b64 %2, %5 offset:512\n\tds_read_b64 %3, %5 offset:1024\n\ts_waitcnt lgkmcnt(0)"
                     : "=&v"(f), "=&v"(o0), "=&v"(o1), "=&v"(o2) : "v"(fl + F_ACCF), "v"(ma) : "memory");
        if (__builtin_amdgcn_readfirstlane(f[0] & f[1] & f[2])) break;
    }
    o = ((o0 + o1) + (o2 + o)) * inv;
    __hip_bfloat162 rr;
    rr.x = __float2bfloat16(o[0]); rr.y = __float2bfloat16(o[1]);
    *reinterpret_cast<__hip_bfloat162*>((char*)out + ooff) = rr;
}

extern "C" void kernel_launch(void* const* d_in, const int* in_sizes, int n_in, void* d_out, int out_size, void* d_ws, size_t ws_size,
                              hipStream_t stream) {
    const __hip_bfloat16* q = (const __hip_bfloat16*)d_in[0];
    const __hip_bfloat16* k = (const __hip_bfloat16*)d_in[1];
    const __hip_bfloat16* v = (const __hip_bfloat16*)d_in[2];
    attn_decode<<<NHEADS, NC * 64 + 64, 0, stream>>>(q, k, v, (__hip_bfloat16*)d_out);
}
```
